```python
import torch

def _softmax(x, axis=-1):
    return torch.softmax(x, dim=axis)
def _gelu(x):
    return torch.nn.functional.gelu(x, approximate="tanh")
def _silu(x):
    return torch.nn.functional.silu(x)
def _sigmoid(x):
    return torch.sigmoid(x)
def _layernorm(x, axis=-1, eps=1e-5):
    m = x.mean(dim=axis, keepdim=True)
    v = x.var(dim=axis, keepdim=True, unbiased=False)
    return (x - m) / torch.sqrt(v + eps)
def _scatter_add(tgt, idx, src, axis=0):
    return tgt.clone().index_add_(axis, idx, src)
def _segment_sum(src, seg, n):
    out = torch.zeros((n,) + src.shape[1:], dtype=src.dtype)
    return out.index_add_(0, seg, src)
def _pad(x, pw):
    flat = tuple(v for pair in reversed(pw) for v in pair)
    return torch.nn.functional.pad(x, flat)

kernel_name = 'gated_mix__mha_block_cached__dense_activation_silu__decode'

def setup_inputs():

    _w = (lambda a: (lambda t: t.to(torch.bfloat16) if t.is_floating_point() else t)(torch.as_tensor(a)))
    def _A_setup():
        B, Sq, D = 2, 1, 512
        H, Sk, Dh = 32, 8192, 128
        return {
            "x": _w((lambda *s: torch.randn(*s, dtype=torch.bfloat16) * 0.05)(B, Sq, D)),
            "k_cache": _w((lambda *s: torch.randn(*s, dtype=torch.bfloat16) * 3.2)(B, H, Sk, Dh)),
            "v_cache": _w((lambda *s: torch.randn(*s, dtype=torch.bfloat16) * 0.1)(B, H, Sk, Dh)),
            "wq": _w((lambda *s: torch.randn(*s, dtype=torch.bfloat16) * 3.2)(D, H * Dh) * (D ** -0.5)),
            "wo": _w((lambda *s: torch.randn(*s, dtype=torch.bfloat16) * 0.1)(H * Dh, D) * ((H * Dh) ** -0.5)),
        }
    _A_in = _A_setup()

    def _B_setup():
        B, S, D, F = 2, 1, 512, 1792
        return {
            "x": _w((lambda *s: torch.randn(*s, dtype=torch.bfloat16) * 0.05)(B, S, D)),
            "w1": _w((lambda *s: torch.randn(*s, dtype=torch.bfloat16) * 0.05)(D, F) * (D ** -0.5)),
            "w2": _w((lambda *s: torch.randn(*s, dtype=torch.bfloat16) * 0.05)(F, D) * (F ** -0.5)),
        }
    _B_in = _B_setup()

    return {
        "A_x": _A_in["x"],
        "A_k_cache": _A_in["k_cache"],
        "A_v_cache": _A_in["v_cache"],
        "A_wq": _A_in["wq"],
        "A_wo": _A_in["wo"],
        "B_x": _B_in["x"],
        "B_w1": _B_in["w1"],
        "B_w2": _B_in["w2"],
        "gate_w": _w((lambda *s: torch.randn(*s, dtype=torch.bfloat16) * 0.05)(512) * 16.0),
    }

def reference(A_x, A_k_cache, A_v_cache, A_wq, A_wo, B_x, B_w1, B_w2, gate_w):
    def _A_ref(x, k_cache, v_cache, wq, wo):
        B, Sq, D = x.shape
        H, Dh = 32, 128
        q = (lambda x, *d: x.permute(*d))((x @ wq).reshape(B, Sq, H, Dh), 0, 2, 1, 3)
        scores = torch.einsum("bhqd,bhkd->bhqk", q, k_cache) * (Dh ** -0.5)
        p = _softmax(scores, axis=-1)
        o = torch.einsum("bhqk,bhkd->bhqd", p, v_cache)
        return (lambda x, *d: x.permute(*d))(o, 0, 2, 1, 3).reshape(B, Sq, H * Dh) @ wo

    def _B_ref(x, w1, w2):
        h = torch.einsum("bsd,df->bsf", x, w1)
        h = _silu(h)
        out = torch.einsum("bsf,fd->bsd", h, w2)
        return out

    x = A_x
    g = _sigmoid((x * gate_w).sum(-1, keepdims=True))
    a = _A_ref(x, A_k_cache, A_v_cache, A_wq, A_wo)
    b = _B_ref(x, B_w1, B_w2)
    return g * a + (1 - g) * b
```

```cpp
#include <hip/hip_runtime.h>
#include <hip/hip_bf16.h>
#include <stdint.h>

typedef unsigned u32x4 __attribute__((ext_vector_type(4)));
typedef __bf16 bf16x2 __attribute__((ext_vector_type(2)));

constexpr int B_ = 2, D_ = 512, H_ = 32, SK = 8192, DH = 128, F_ = 1792;
constexpr int NSPLIT = 8;
constexpr int KEYS = SK / NSPLIT;

__device__ __forceinline__ float bf2f(unsigned short u) { return __uint_as_float(((unsigned)u) << 16); }
__device__ __forceinline__ float lo16(unsigned u) { return __uint_as_float(u << 16); }
__device__ __forceinline__ float hi16(unsigned u) { return __uint_as_float(u & 0xffff0000u); }
__device__ __forceinline__ float dot2(unsigned k, unsigned q, float acc) {
  return __builtin_amdgcn_fdot2_f32_bf16(__builtin_bit_cast(bf16x2, k), __builtin_bit_cast(bf16x2, q), acc, false);
}
template <int CTRL> __device__ __forceinline__ float dpp(float x) {
  return __builtin_bit_cast(float, __builtin_amdgcn_mov_dpp(__builtin_bit_cast(int, x), CTRL, 0xf, 0xf, true));
}
__device__ __forceinline__ float row16_sum(float x) {
  x += dpp<0xB1>(x); x += dpp<0x4E>(x); x += dpp<0x141>(x); x += dpp<0x128>(x); return x;
}

__global__ void __launch_bounds__(256) k1(const unsigned short* x, const unsigned short* wq, const unsigned short* w1,
                                           unsigned short* q, float* hbuf) {
  int col = blockIdx.x * 256 + threadIdx.x;
  __shared__ float xs[2][512];
  for (int i = threadIdx.x; i < 1024; i += 256) xs[i / 512][i % 512] = bf2f(x[i]);
  __syncthreads();
  if (col < 4096) {
    float a0 = 0, a1 = 0;
    for (int k = 0; k < 512; ++k) { float w = bf2f(wq[k * 4096 + col]); a0 += xs[0][k] * w; a1 += xs[1][k] * w; }
    __hip_bfloat16 b0 = __float2bfloat16(a0), b1 = __float2bfloat16(a1);
    q[col] = *(unsigned short*)&b0; q[4096 + col] = *(unsigned short*)&b1;
  } else if (col < 4096 + F_) {
    int f = col - 4096;
    float a0 = 0, a1 = 0;
    for (int k = 0; k < 512; ++k) { float w = bf2f(w1[k * F_ + f]); a0 += xs[0][k] * w; a1 += xs[1][k] * w; }
    hbuf[f] = a0 / (1.f + __expf(-a0)); hbuf[F_ + f] = a1 / (1.f + __expf(-a1));
  }
}

__global__ void __launch_bounds__(256) k2(const unsigned short* q, const unsigned short* K, const unsigned short* V, float* part) {
  int s = blockIdx.x, bh = blockIdx.y;
  int tid = threadIdx.x, lane = tid & 63, wave = tid >> 6, r = lane >> 4, c = lane & 15;
  const float C2 = 0.08838834764831845f * 1.4426950408889634f;
  u32x4 qp = *(const u32x4*)(q + bh * 128 + 8 * c);
  const unsigned short* kb = K + ((size_t)bh * SK + s * KEYS) * DH;
  const unsigned short* vb = V + ((size_t)bh * SK + s * KEYS) * DH;
  float m = -INFINITY, l = 0, acc[8] = {0, 0, 0, 0, 0, 0, 0, 0};
  for (int t0 = wave * 16; t0 < KEYS; t0 += 64) {
    u32x4 kk[4], vv[4];
#pragma unroll
    for (int i = 0; i < 4; ++i) {
      kk[i] = __builtin_nontemporal_load((const u32x4*)(kb + (size_t)(t0 + 4 * i + r) * DH + 8 * c));
      vv[i] = __builtin_nontemporal_load((const u32x4*)(vb + (size_t)(t0 + 4 * i + r) * DH + 8 * c));
    }
    float sc[4];
#pragma unroll
    for (int i = 0; i < 4; ++i) {
      float d = 0;
#pragma unroll
      for (int j = 0; j < 4; ++j) d = dot2(kk[i][j], qp[j], d);
      sc[i] = row16_sum(d) * C2;
    }
    float mn = fmaxf(fmaxf(fmaxf(sc[0], sc[1]), fmaxf(sc[2], sc[3])), m);
    float al = __builtin_amdgcn_exp2f(m - mn);
    m = mn; l *= al;
#pragma unroll
    for (int j = 0; j < 8; ++j) acc[j] *= al;
#pragma unroll
    for (int i = 0; i < 4; ++i) {
      float p = __builtin_amdgcn_exp2f(sc[i] - mn);
      l += p;
#pragma unroll
      for (int j = 0; j < 4; ++j) { acc[2 * j] += p * lo16(vv[i][j]); acc[2 * j + 1] += p * hi16(vv[i][j]); }
    }
  }
  __shared__ float sm[16][132];
  int g = wave * 4 + r;
#pragma unroll
  for (int j = 0; j < 8; ++j) sm[g][8 * c + j] = acc[j];
  if (c == 0) { sm[g][128] = m; sm[g][129] = l; }
  __syncthreads();
  if (tid < 128) {
    float M = -INFINITY;
    for (int i = 0; i < 16; ++i) M = fmaxf(M, sm[i][128]);
    float L = 0, o = 0;
    for (int i = 0; i < 16; ++i) { float w = __builtin_amdgcn_exp2f(sm[i][128] - M); L += w * sm[i][129]; o += w * sm[i][tid]; }
    float* pp = part + ((size_t)bh * NSPLIT + s) * 132;
    pp[tid] = o;
    if (tid == 0) { pp[128] = M; pp[129] = L; }
  }
}

__global__ void __launch_bounds__(256) k3(const unsigned short* x, const unsigned short* gw, const float* part, const float* hbuf,
                                           const unsigned short* wo, const unsigned short* w2, __hip_bfloat16* out) {
  __shared__ float o[2][4096];
  __shared__ float red[256][16];
  __shared__ float gs[2];
  int tid = threadIdx.x;
  for (int i = tid; i < 8192; i += 256) {
    int bh = i >> 7, d = i & 127;
    const float* pp = part + (size_t)bh * NSPLIT * 132;
    float M = -INFINITY;
    for (int s = 0; s < NSPLIT; ++s) M = fmaxf(M, pp[s * 132 + 128]);
    float L = 0, a = 0;
    for (int s = 0; s < NSPLIT; ++s) { float w = __builtin_amdgcn_exp2f(pp[s * 132 + 128] - M); L += w * pp[s * 132 + 129]; a += w * pp[s * 132 + d]; }
    o[bh >> 5][(bh & 31) * 128 + d] = a / L;
  }
  if (tid < 2) { float a = 0; for (int k = 0; k < 512; ++k) a += bf2f(x[tid * 512 + k]) * bf2f(gw[k]); gs[tid] = 1.f / (1.f + __expf(-a)); }
  __syncthreads();
  int c0 = blockIdx.x * 8;
  float a0[8] = {0}, a1[8] = {0}, f0[8] = {0}, f1[8] = {0};
  for (int k = tid; k < 4096; k += 256) {
    u32x4 w = *(const u32x4*)(wo + (size_t)k * 512 + c0);
#pragma unroll
    for (int j = 0; j < 4; ++j) { a0[2*j] += o[0][k] * lo16(w[j]); a0[2*j+1] += o[0][k] * hi16(w[j]); a1[2*j] += o[1][k] * lo16(w[j]); a1[2*j+1] += o[1][k] * hi16(w[j]); }
  }
  for (int k = tid; k < F_; k += 256) {
    u32x4 w = *(const u32x4*)(w2 + (size_t)k * 512 + c0);
    float h0 = hbuf[k], h1 = hbuf[F_ + k];
#pragma unroll
    for (int j = 0; j < 4; ++j) { f0[2*j] += h0 * lo16(w[j]); f0[2*j+1] += h0 * hi16(w[j]); f1[2*j] += h1 * lo16(w[j]); f1[2*j+1] += h1 * hi16(w[j]); }
  }
#pragma unroll
  for (int j = 0; j < 8; ++j) { red[tid][j] = gs[0] * a0[j] + (1.f - gs[0]) * f0[j]; red[tid][8 + j] = gs[1] * a1[j] + (1.f - gs[1]) * f1[j]; }
  __syncthreads();
  if (tid < 16) {
    float a = 0;
    for (int i = 0; i < 256; ++i) a += red[i][tid];
    out[(tid >> 3) * 512 + c0 + (tid & 7)] = __float2bfloat16(a);
  }
}

extern "C" void kernel_launch(void* const* d_in, const int* in_sizes, int n_in, void* d_out, int out_size, void* d_ws, size_t ws_size,
                              hipStream_t stream) {
  const unsigned short* x = (const unsigned short*)d_in[0];
  const unsigned short* K = (const unsigned short*)d_in[1];
  const unsigned short* V = (const unsigned short*)d_in[2];
  const unsigned short* wq = (const unsigned short*)d_in[3];
  const unsigned short* wo = (const unsigned short*)d_in[4];
  const unsigned short* w1 = (const unsigned short*)d_in[6];
  const unsigned short* w2 = (const unsigned short*)d_in[7];
  const unsigned short* gw = (const unsigned short*)d_in[8];
  char* ws = (char*)d_ws;
  unsigned short* q = (unsigned short*)ws;
  float* hbuf = (float*)(ws + 65536);
  float* part = (float*)(ws + 131072);
  k1<<<23, 256, 0, stream>>>(x, wq, w1, q, hbuf);
  k2<<<dim3(NSPLIT, 64), 256, 0, stream>>>(q, K, V, part);
  k3<<<64, 256, 0, stream>>>(x, gw, part, hbuf, wo, w2, (__hip_bfloat16*)d_out);
}
```

```cpp
#include <hip/hip_runtime.h>
#include <hip/hip_bf16.h>
#include <stdint.h>

#pragma clang fp contract(off)

typedef unsigned u32x4 __attribute__((ext_vector_type(4)));
typedef unsigned u32x2 __attribute__((ext_vector_type(2)));
typedef float f32x4 __attribute__((ext_vector_type(4)));
typedef float f32x2 __attribute__((ext_vector_type(2)));
typedef __bf16 bf16x2 __attribute__((ext_vector_type(2)));
#define GAS __attribute__((address_space(1)))

constexpr int D_MODEL = 512, NH = 32, SK = 8192, DH = 128, FF = 1792;
constexpr int NSW = 4, NHW = 8, NWAVE = NSW + NHW, NTHR = NWAVE * 64;
constexpr int TL = 2;
constexpr int NB = 8;
constexpr int DF = 3;
constexpr int TRN = NB - 2;
constexpr int STEP_EL = NSW * TL * 4 * DH;
constexpr int NCHUNK = SK / 64;
constexpr int STAT = 29;
constexpr int NDYN = NCHUNK - 4 * STAT;
constexpr int AHEAD = 1;
constexpr int FSL = FF / NH;
static_assert(NB % 2 == 0 && DF <= NB && NB - 1 <= STAT && NDYN >= 0 && NDYN <= 12, "<= 16 partial slots per (b,h): 4 static + NDYN dynamic");
constexpr int SPW = (4 + NDYN) / 2;
constexpr int NSEG = NDYN + 1;

constexpr size_t WS_EPOCH = 0;
constexpr unsigned WS_CTR = 4096;
constexpr unsigned WS_QP = 256u << 10;
constexpr unsigned WS_HP = 512u << 10;
constexpr unsigned WS_FIN = 1u << 20;
constexpr unsigned WS_S1 = 2u << 20;
constexpr unsigned WS_BYTES = 4u << 20;
constexpr unsigned SPIN_CAP = 1u << 17;

template <int I> struct IC { static constexpr int v = I; };
template <int I, int N, typename F> __device__ __forceinline__ void static_for(F&& f) {
  if constexpr (I < N) { f(IC<I>{}); static_for<I + 1, N>(f); }
}
template <int N> __device__ __forceinline__ void wait_vm() {
  __builtin_amdgcn_s_waitcnt((N & 15) | ((N >> 4) << 14) | (7 << 4) | (15 << 8));
}
__device__ __forceinline__ float lo16(unsigned u) { return __uint_as_float(u << 16); }
__device__ __forceinline__ float hi16(unsigned u) { return __uint_as_float(u & 0xffff0000u); }
__device__ __forceinline__ float dot2(unsigned k, unsigned q, float acc) {
  return __builtin_amdgcn_fdot2_f32_bf16(__builtin_bit_cast(bf16x2, k), __builtin_bit_cast(bf16x2, q), acc, false);
}
template <int CTRL> __device__ __forceinline__ float dpp(float x) {
  return __builtin_bit_cast(float, __builtin_amdgcn_mov_dpp(__builtin_bit_cast(int, x), CTRL, 0xf, 0xf, true));
}
constexpr int XOR1 = 0xB1, XOR2 = 0x4E, XOR7 = 0x141, ROR8 = 0x128;
__device__ __forceinline__ float row16_sum(float x) {
  x += dpp<XOR1>(x); x += dpp<XOR2>(x); x += dpp<XOR7>(x); x += dpp<ROR8>(x); return x;
}
__device__ __forceinline__ float tr16(float a, float b) {
  auto s = __builtin_amdgcn_permlane16_swap(__float_as_uint(a), __float_as_uint(b), false, false);
  return __uint_as_float(s[0]) + __uint_as_float(s[1]);
}
__device__ __forceinline__ float tr32(float a, float b) {
  auto s = __builtin_amdgcn_permlane32_swap(__float_as_uint(a), __float_as_uint(b), false, false);
  return __uint_as_float(s[0]) + __uint_as_float(s[1]);
}
__device__ __forceinline__ float xrow_sum(float x) { x = tr16(x, x); return tr32(x, x); }
__device__ __forceinline__ float xrow_max(float x) {
  auto s = __builtin_amdgcn_permlane16_swap(__float_as_uint(x), __float_as_uint(x), false, false);
  x = fmaxf(__uint_as_float(s[0]), __uint_as_float(s[1]));
  auto t = __builtin_amdgcn_permlane32_swap(__float_as_uint(x), __float_as_uint(x), false, false);
  return fmaxf(__uint_as_float(t[0]), __uint_as_float(t[1]));
}
__device__ __forceinline__ f32x2 tr8(const float (&v)[8]) {
  const float t0 = tr16(v[0], v[4]), t1 = tr16(v[1], v[5]), t2 = tr16(v[2], v[6]), t3 = tr16(v[3], v[7]);
  return f32x2{tr32(t0, t2), tr32(t1, t3)};
}
__device__ __forceinline__ f32x2 fma2(f32x2 a, float b, f32x2 c) { return __builtin_elementwise_fma(a, f32x2{b, b}, c); }
__device__ __forceinline__ unsigned pack_bf16(float lo, float hi) {
  unsigned r; asm volatile("v_cvt_pk_bf16_f32 %0, %1, %2" : "=v"(r) : "v"(lo), "v"(hi)); return r;
}
__device__ __forceinline__ u32x4 ldnt(const void* p) { return __builtin_nontemporal_load((const u32x4*)p); }
__device__ __forceinline__ u32x4 ld16(const void* p) { return *(const u32x4*)p; }
__device__ __forceinline__ unsigned lds_ld(const unsigned* p) { return __hip_atomic_load(p, __ATOMIC_RELAXED, __HIP_MEMORY_SCOPE_WORKGROUP); }
__device__ __forceinline__ void lds_st(unsigned* p, unsigned v) { __hip_atomic_store(p, v, __ATOMIC_RELAXED, __HIP_MEMORY_SCOPE_WORKGROUP); }
#define REL_WG() __builtin_amdgcn_fence(__ATOMIC_RELEASE, "workgroup")
#define ACQ_WG() __builtin_amdgcn_fence(__ATOMIC_ACQUIRE, "workgroup")
#define PUT(off_, v0_, v1_) __builtin_amdgcn_raw_buffer_store_b128(u32x4{__float_as_uint(v0_), tagA, __float_as_uint(v1_), tagB}, wsb, (off_), 0, 16)
#define GET(off_) __builtin_amdgcn_raw_buffer_load_b128(wsb, (off_), 0, 16)
#define BAD(g_) (((g_)[1] ^ tagA) | ((g_)[3] ^ tagB))
#define EMPTY (u32x4{0u, ~tagA, 0u, 0u})

struct Params {
  const unsigned short* x; const unsigned short* K; const unsigned short* V; const unsigned short* wq; const unsigned short* wo;
  const unsigned short* w1; const unsigned short* w2; const unsigned short* gw; unsigned short* out; unsigned char* ws;
};

constexpr int L_Q = 0;
constexpr int L_QF = L_Q + NHW * 256;
constexpr int L_HC = L_QF + 64;
constexpr int SEG_W = NSW * 128 + 8;
constexpr int L_SEG = L_HC + 32;
constexpr int L_G = L_SEG + NSEG * SEG_W;
constexpr int L_O = L_G + NSW * 64;
constexpr int L_FIN = L_O + 64;
constexpr int L_W = L_FIN + 64;
constexpr int L_SC = L_W + 16;
constexpr int L_DQ = L_SC + 16;
constexpr int NDQ = 16;
constexpr int L_TOTAL = L_DQ + NDQ;
enum { W_QCNT, W_HCNT, W_QFLAG, W_HFLAG, W_OFLAG, W_FCNT, W_LATE, W_PROG, W_GCNT, W_TFLAG, W_TAGA, W_TAGB, W_G0, W_G1 };
constexpr unsigned DQ_NONE = 0xffffffffu, DQ_END = 0xfffffffeu;
static_assert(NDYN + 1 <= NDQ, "");

__global__ void __launch_bounds__(NTHR, NWAVE / 4) fused_decode_kernel(Params p) {
  __shared__ __attribute__((aligned(16))) float lds[L_TOTAL];
  unsigned* const W = (unsigned*)&lds[L_W];
  unsigned* const DQ = (unsigned*)&lds[L_DQ];
  unsigned* const SC = (unsigned*)&lds[L_SC];
  const int tid = threadIdx.x, lane = tid & 63, wave = __builtin_amdgcn_readfirstlane(tid >> 6);
  const int r = lane >> 4, c = lane & 15;
  const int g8 = lane >> 3, c8 = lane & 7;
  const int bid = blockIdx.x;
  const int h = bid >> 3, rot = (bid - h) & 7;
  const int b = (rot >> 1) & 1, s = (rot & 1) | ((rot >> 2) << 1);
  const int j8 = b * 4 + s;
  const bool powner = (j8 & 1) != 0;
  const int ow = j8 >> 1;
  const __amdgpu_buffer_rsrc_t wsb = __builtin_amdgcn_make_buffer_rsrc(p.ws, 0, WS_BYTES, 0x00020000);
  auto publish_seg = [&](int seg, unsigned slot, unsigned tagA, unsigned tagB) {
    const float* rec = &lds[L_SEG + seg * SEG_W];
    float M = -INFINITY, L = 0.f;
    f32x2 ml[NSW], a[NSW];
#pragma unroll
    for (int w = 0; w < NSW; ++w) { ml[w] = *(const f32x2*)&rec[NSW * 128 + 2 * w]; a[w] = *(const f32x2*)&rec[w * 128 + 2 * lane]; }
#pragma unroll
    for (int w = 0; w < NSW; ++w) M = fmaxf(M, ml[w][0]);
    f32x2 o = {0.f, 0.f};
#pragma unroll
    for (int w = 0; w < NSW; ++w) { const float e = __builtin_amdgcn_exp2f(ml[w][0] - M); L = fmaf(e, ml[w][1], L); o = fma2(a[w], e, o); }
    const unsigned po = WS_S1 + (unsigned)((h * 32 + b * 16) * 2048 + lane * 16) + slot * 2048;
    PUT(po, o[0], o[1]);
    if (lane < 8) PUT(po + 1024, M, L);
  };
  if (wave >= NHW) {
    const int sw = wave - NHW;
    const size_t kvbase = ((size_t)(b * NH + h) * SK) * DH + (size_t)(sw * TL * 4 + r) * DH + 8 * c;
    const unsigned short* kp = p.K + kvbase;
    const unsigned short* vp = p.V + kvbase;
    u32x4 bk[NB][TL], bv[NB][TL];
#define ISSUE(d_, step_) do { const size_t o_ = (size_t)(step_) * STEP_EL; const unsigned short* k_ = kp + o_; const unsigned short* v_ = vp + o_; \
    _Pragma("unroll") for (int t_ = 0; t_ < TL; ++t_) bk[d_][t_] = ldnt(k_ + t_ * 4 * DH); \
    _Pragma("unroll") for (int t_ = 0; t_ < TL; ++t_) bv[d_][t_] = ldnt(v_ + t_ * 4 * DH); \
    __builtin_amdgcn_sched_barrier(0); } while (0)
#define SSTEP(t_) ((((t_) >> 1) * 4 + s) * 2 + ((t_) & 1))
    __syncthreads();
    static_for<0, NB>([&](auto ic) { constexpr int k = decltype(ic)::v;
      if constexpr (k >= DF) { wait_vm<(DF - 1) * 2 * TL>(); __builtin_amdgcn_sched_barrier(0); }
      ISSUE(k, SSTEP(k)); });
    while (lds_ld(W + W_QFLAG) == 0u) __builtin_amdgcn_s_sleep(1);
    ACQ_WG();
    const u32x4 qp = *(const u32x4*)&lds[L_QF + 4 * c];
    constexpr float C2 = 0.08838834764831845f * 1.4426950408889634f;
    float m = -INFINITY, l = 0.f, acc[8];
#pragma unroll
    for (int j = 0; j < 8; ++j) acc[j] = 0.f;
#define CONSUME(d_) do { \
    float sc_[TL]; \
    _Pragma("unroll") for (int t_ = 0; t_ < TL; ++t_) { float dd = 0.f; \
      _Pragma("unroll") for (int j = 0; j < 4; ++j) dd = dot2(bk[d_][t_][j], qp[j], dd); \
      sc_[t_] = row16_sum(dd) * C2; } \
    float mn = m; \
    _Pragma("unroll") for (int t_ = 0; t_ < TL; ++t_) mn = fmaxf(mn, sc_[t_]); \
    const float al = __builtin_amdgcn_exp2f(m - mn); m = mn; l *= al; \
    _Pragma("unroll") for (int j = 0; j < 8; ++j) acc[j] *= al; \
    _Pragma("unroll") for (int t_ = 0; t_ < TL; ++t_) { const float pp = __builtin_amdgcn_exp2f(sc_[t_] - mn); l += pp; \
      _Pragma("unroll") for (int j = 0; j < 4; ++j) { acc[2 * j] = fmaf(pp, lo16(bv[d_][t_][j]), acc[2 * j]); acc[2 * j + 1] = fmaf(pp, hi16(bv[d_][t_][j]), acc[2 * j + 1]); } } \
    _Pragma("unroll") for (int j = 0; j < 8; ++j) asm volatile("" : "+v"(acc[j])); \
    __builtin_amdgcn_sched_barrier(0); \
  } while (0)
    static_for<0, TRN>([&](auto ic) { constexpr int k = decltype(ic)::v;
      CONSUME((2 * k) % NB); ISSUE(k, SSTEP(NB + k)); CONSUME((2 * k + 1) % NB); });
    constexpr int BA = (2 * TRN) % NB, BB = (2 * TRN + 1) % NB;
#define FLUSH(seg_) do { \
      const float mw = xrow_max(m); \
      const float scl = __builtin_amdgcn_exp2f(m - mw); \
      const float lw = xrow_sum(l * scl); \
      _Pragma("unroll") for (int j = 0; j < 8; ++j) acc[j] *= scl; \
      float* rec = &lds[L_SEG + (seg_) * SEG_W]; \
      *(f32x2*)&rec[sw * 128 + 8 * c + 4 * (r & 1) + 2 * (r >> 1)] = tr8(acc); \
      *(f32x2*)&rec[NSW * 128 + 2 * sw] = f32x2{mw, lw}; \
      REL_WG(); \
      if (lane == 0) arr = __hip_atomic_fetch_add(SC + (seg_), 1u, __ATOMIC_RELAXED, __HIP_MEMORY_SCOPE_WORKGROUP); \
      m = -INFINITY; l = 0.f; \
      _Pragma("unroll") for (int j = 0; j < 8; ++j) acc[j] = 0.f; \
      __builtin_amdgcn_sched_barrier(0); \
    } while (0)
    unsigned arr = 0u, slot = (unsigned)s, nslot = (unsigned)s;
    int i = TRN + 1;
    for (;; ++i) {
      lds_st(W + W_PROG, (unsigned)i);
      unsigned cid = (unsigned)(i * 4 + s);
      if (i >= STAT) {
        unsigned v;
        while ((v = lds_ld(DQ + (i - STAT))) == DQ_NONE) {}
        v = __builtin_amdgcn_readfirstlane(v);
        if (v == DQ_END) break;
        cid = v; nslot = 4u + v - (unsigned)(4 * STAT);
      }
      CONSUME(BA); ISSUE(BA, 2 * cid);
      CONSUME(BB); ISSUE(BB, 2 * cid + 1);
      if (i >= STAT) FLUSH(i - STAT);
      slot = nslot;
    }
    while (lds_ld(W + W_TFLAG) == 0u) __builtin_amdgcn_s_sleep(1);
    ACQ_WG();
    const unsigned tagA = __builtin_amdgcn_readfirstlane(lds_ld(W + W_TAGA)), tagB = __builtin_amdgcn_readfirstlane(lds_ld(W + W_TAGB));
    CONSUME(BA); CONSUME(BB);
    FLUSH(i - STAT);
    if (__builtin_amdgcn_readfirstlane(arr) == (unsigned)(NSW - 1)) {
      ACQ_WG();
      publish_seg(i - STAT, slot, tagA, tagB);
    }
    if (powner) {
      const float gsel = lds[L_W + W_G0 + (c8 >> 2)];
      const unsigned rec = WS_S1 + (unsigned)((h * 32 + (sw >> 1) * 16 + (sw & 1) * SPW + c8) * 2048);
      const unsigned offa = rec + (unsigned)(ow * 256 + g8 * 32), offm = rec + (unsigned)(1024 + g8 * 16);
      u32x4 ga = EMPTY, gb = EMPTY, gm = EMPTY;
      if (c8 >= SPW) { ga = u32x4{0u, tagA, 0u, tagB}; gb = ga; gm = u32x4{__float_as_uint(-3.0e38f), tagA, 0u, tagB}; }
      for (unsigned spins = 0;; ++spins) {
        asm volatile("" ::: "memory");
        if ((BAD(ga) | BAD(gb) | BAD(gm)) != 0u) { ga = GET(offa); gb = GET(offa + 16); gm = GET(offm); }
        if (__all((BAD(ga) | BAD(gb) | BAD(gm)) == 0u) || spins > SPIN_CAP) break;
      }
      {
        const float pm = __uint_as_float(gm[0]), pl = __uint_as_float(gm[2]);
        float Mx = fmaxf(pm, dpp<XOR1>(pm)); Mx = fmaxf(Mx, dpp<XOR2>(Mx)); Mx = fmaxf(Mx, dpp<XOR7>(Mx));
        const float ee = __builtin_amdgcn_exp2f(pm - Mx);
        float Ls = ee * pl, o0 = ee * __uint_as_float(ga[0]), o1 = ee * __uint_as_float(ga[2]), o2 = ee * __uint_as_float(gb[0]), o3 = ee * __uint_as_float(gb[2]);
        Ls += dpp<XOR1>(Ls); o0 += dpp<XOR1>(o0); o1 += dpp<XOR1>(o1); o2 += dpp<XOR1>(o2); o3 += dpp<XOR1>(o3);
        Ls += dpp<XOR2>(Ls); o0 += dpp<XOR2>(o0); o1 += dpp<XOR2>(o1); o2 += dpp<XOR2>(o2); o3 += dpp<XOR2>(o3);
        Ls += dpp<XOR7>(Ls); o0 += dpp<XOR7>(o0); o1 += dpp<XOR7>(o1); o2 += dpp<XOR7>(o2); o3 += dpp<XOR7>(o3);
        *(f32x4*)&lds[L_G + sw * 64 + 8 * g8 + 4 * (c8 & 1)] = (c8 & 1) ? f32x4{o0, o1, o2, o3} : f32x4{Mx, Ls, 0.f, 0.f};
      }
      REL_WG();
      arr = 0u;
      if (lane == 0) arr = __hip_atomic_fetch_add(W + W_GCNT, 1u, __ATOMIC_RELAXED, __HIP_MEMORY_SCOPE_WORKGROUP);
      if (__builtin_amdgcn_readfirstlane(arr) == (unsigned)(NSW - 1)) {
        ACQ_WG();
        const float* g0p = &lds[L_G + (c8 >> 2) * 128 + 8 * g8];
        const f32x2 mla = *(const f32x2*)g0p, mlb = *(const f32x2*)(g0p + 64);
        const f32x4 oa = *(const f32x4*)(g0p + 4), ob = *(const f32x4*)(g0p + 68);
        const float Mx = fmaxf(mla[0], mlb[0]);
        const float ea = __builtin_amdgcn_exp2f(mla[0] - Mx), eb = __builtin_amdgcn_exp2f(mlb[0] - Mx);
        const float sc = gsel * __builtin_amdgcn_rcpf(ea * mla[1] + eb * mlb[1]);
        *(f32x4*)&lds[L_O + 8 * g8 + 4 * (c8 >> 2)] = (oa * ea + ob * eb) * sc;
        REL_WG();
        lds_st(W + W_OFLAG, 1u);
      }
    } else if (s == 0) {
      const int fo = h * 2 + b;
      const unsigned off = WS_FIN + (unsigned)((sw * 32 + g8) * 8192 + fo * 128 + c8 * 16);
      u32x4 g[4];
#pragma unroll
      for (int k = 0; k < 4; ++k) g[k] = EMPTY;
      for (unsigned spins = 0;; ++spins) {
        asm volatile("" ::: "memory");
        unsigned bad = 0u;
#pragma unroll
        for (int k = 0; k < 4; ++k) { if (BAD(g[k]) != 0u) g[k] = GET(off + k * 65536); }
#pragma unroll
        for (int k = 0; k < 4; ++k) bad |= BAD(g[k]);
        if (__all(bad == 0u) || spins > SPIN_CAP) break;
      }
      float a0 = 0.f, a1 = 0.f;
#pragma unroll
      for (int k = 0; k < 4; ++k) { a0 += __uint_as_float(g[k][0]); a1 += __uint_as_float(g[k][2]); }
      a0 += dpp<ROR8>(a0); a1 += dpp<ROR8>(a1);
      a0 = tr16(a0, a1); a0 = tr32(a0, a0);
      lds[L_FIN + sw * 16 + 2 * c8 + (r & 1)] = a0;
      REL_WG();
      arr = 0u;
      if (lane == 0) arr = __hip_atomic_fetch_add(W + W_FCNT, 1u, __ATOMIC_RELAXED, __HIP_MEMORY_SCOPE_WORKGROUP);
      if (__builtin_amdgcn_readfirstlane(arr) == (unsigned)(NSW - 1)) {
        ACQ_WG();
        f32x2 a = {0.f, 0.f};
#pragma unroll
        for (int w = 0; w < NSW; ++w) a += *(const f32x2*)&lds[L_FIN + w * 16 + 2 * c8];
        const unsigned pk = pack_bf16(a[0], a[1]);
        if (lane < 8) {
          *(unsigned*)(p.out + fo * 16 + 2 * lane) = pk;
          if (fo == 0 && lane == 0) __hip_atomic_store((GAS unsigned long long*)(p.ws + WS_EPOCH), ((unsigned long long)tagB << 32) | tagA, __ATOMIC_RELAXED, __HIP_MEMORY_SCOPE_AGENT);
        }
      }
    }
  } else {
    const int hw = wave;
    const unsigned long long e0 = __hip_atomic_load((GAS unsigned long long*)(p.ws + WS_EPOCH), __ATOMIC_RELAXED, __HIP_MEMORY_SCOPE_AGENT);
    const int qrow = 128 * s + 16 * hw + 2 * g8;
    const unsigned short* wqp = p.wq + (size_t)qrow * (NH * DH) + h * DH + 64 * b + 8 * c8;
    u32x4 wq0 = ldnt(wqp), wq1 = ldnt(wqp + NH * DH);
    const unsigned xq0 = *(const unsigned*)(p.x + qrow), xq1 = *(const unsigned*)(p.x + D_MODEL + qrow);
    const int hrow = 64 * j8 + 8 * hw + g8;
    u32x4 w1r = ldnt(p.w1 + (size_t)hrow * FF + h * FSL + 8 * (c8 < 7 ? c8 : 6));
    const unsigned short xh0 = p.x[hrow], xh1 = p.x[D_MODEL + hrow];
    const u32x4 xg0 = ld16(p.x + 8 * lane), xg1 = ld16(p.x + D_MODEL + 8 * lane), gwv = ld16(p.gw + 8 * lane);
    __builtin_amdgcn_sched_barrier(0);
    if (tid < 32) W[tid] = 0u;
    if (tid >= 64 && tid < 64 + NDQ) DQ[tid - 64] = DQ_NONE;
    __syncthreads();
    asm volatile("" : "+v"(wq0), "+v"(wq1));
    {
      float qb[8];
#pragma unroll
      for (int j = 0; j < 4; ++j) {
        const float w00 = lo16(wq0[j]), w01 = hi16(wq0[j]), w10 = lo16(wq1[j]), w11 = hi16(wq1[j]);
        const float a0 = fmaf(lo16(xq0), w00, hi16(xq0) * w10), a1 = fmaf(lo16(xq0), w01, hi16(xq0) * w11);
        const float b0 = fmaf(lo16(xq1), w00, hi16(xq1) * w10), b1 = fmaf(lo16(xq1), w01, hi16(xq1) * w11);
        const float t0 = a0 + dpp<ROR8>(a0), t1 = b0 + dpp<ROR8>(b0), t2 = a1 + dpp<ROR8>(a1), t3 = b1 + dpp<ROR8>(b1);
        qb[2 * j] = (lane & 8) ? t1 : t0; qb[2 * j + 1] = (lane & 8) ? t3 : t2;
      }
      *(f32x2*)&lds[L_Q + hw * 256 + 8 * c + 4 * (r & 1) + 2 * (r >> 1)] = tr8(qb);
    }
    REL_WG();
    if (lane == 0) __hip_atomic_fetch_add(W + W_QCNT, 1u, __ATOMIC_RELAXED, __HIP_MEMORY_SCOPE_WORKGROUP);
    const unsigned tagA = (unsigned)e0 + 1u, tagB = (unsigned)(e0 >> 32) + 1u;
    if (hw == 0) {
      while (lds_ld(W + W_QCNT) < (unsigned)NHW) {}
      ACQ_WG();
      f32x2 v = {0.f, 0.f};
#pragma unroll
      for (int w = 0; w < NHW; ++w) v += *(const f32x2*)&lds[L_Q + w * 256 + 2 * lane];
      PUT(WS_QP + (unsigned)((((lane >> 5) * NH + h) * 4 + s) * 1024 + (32 * b + (lane & 31)) * 16), v[0], v[1]);
    }
    asm volatile("" : "+v"(w1r));
    {
      float hb[8];
      const float x0 = lo16(xh0), x1 = lo16(xh1);
#pragma unroll
      for (int j = 0; j < 4; ++j) {
        const float a0 = x0 * lo16(w1r[j]), a1 = x0 * hi16(w1r[j]), b0 = x1 * lo16(w1r[j]), b1 = x1 * hi16(w1r[j]);
        const float t0 = a0 + dpp<ROR8>(a0), t1 = b0 + dpp<ROR8>(b0), t2 = a1 + dpp<ROR8>(a1), t3 = b1 + dpp<ROR8>(b1);
        hb[2 * j] = (lane & 8) ? t1 : t0; hb[2 * j + 1] = (lane & 8) ? t3 : t2;
      }
      *(f32x2*)&lds[L_Q + hw * 256 + 128 + 8 * c + 4 * (r & 1) + 2 * (r >> 1)] = tr8(hb);
    }
    REL_WG();
    if (lane == 0) __hip_atomic_fetch_add(W + W_HCNT, 1u, __ATOMIC_RELAXED, __HIP_MEMORY_SCOPE_WORKGROUP);
    if (hw == 1) {
      while (lds_ld(W + W_QCNT) < (unsigned)NHW) {}
      __builtin_amdgcn_s_sleep(10);
      const unsigned off = WS_QP + (unsigned)(((b * NH + h) * 4 + r) * 1024 + c * 64);
      u32x4 q4[4];
#pragma unroll
      for (int k = 0; k < 4; ++k) q4[k] = EMPTY;
      for (unsigned spins = 0;; ++spins) {
        asm volatile("" ::: "memory");
        if ((BAD(q4[0]) | BAD(q4[1]) | BAD(q4[2]) | BAD(q4[3])) != 0u) {
#pragma unroll
          for (int k = 0; k < 4; ++k) q4[k] = GET(off + 16 * k);
        }
        if (__all((BAD(q4[0]) | BAD(q4[1]) | BAD(q4[2]) | BAD(q4[3])) == 0u) || spins > SPIN_CAP) break;
      }
      {
        u32x4 qo;
#pragma unroll
        for (int k = 0; k < 4; ++k) qo[k] = pack_bf16(xrow_sum(__uint_as_float(q4[k][0])), xrow_sum(__uint_as_float(q4[k][2])));
        *(u32x4*)&lds[L_QF + 4 * c] = qo;
        REL_WG();
        lds_st(W + W_QFLAG, 1u);
      }
    } else if (hw == NHW - 1) {
      float g0 = 0.f, g1 = 0.f;
#pragma unroll
      for (int j = 0; j < 4; ++j) { g0 = dot2(xg0[j], gwv[j], g0); g1 = dot2(xg1[j], gwv[j], g1); }
      g0 = xrow_sum(row16_sum(g0)); g1 = xrow_sum(row16_sum(g1));
      g0 = 1.f / (1.f + __expf(-g0)); g1 = 1.f / (1.f + __expf(-g1));
      lds[L_W + W_G0] = g0; lds[L_W + W_G1] = g1;
      W[W_TAGA] = tagA; W[W_TAGB] = tagB;
      REL_WG();
      lds_st(W + W_TFLAG, 1u);
      while (lds_ld(W + W_HCNT) < (unsigned)NHW) __builtin_amdgcn_s_sleep(1);
      ACQ_WG();
      {
        const int col = 14 * r + (c < 14 ? c : 13);
        float v0 = 0.f, v1 = 0.f;
#pragma unroll
        for (int w = 0; w < NHW; ++w) { v0 += lds[L_Q + w * 256 + 128 + col]; v1 += lds[L_Q + w * 256 + 128 + 64 + col]; }
        PUT(WS_HP + (unsigned)(((h * 4 + r) * 8 + j8) * 256 + c * 16), v0, v1);
      }
      if (powner) {
        while (lds_ld(W + W_QFLAG) == 0u) __builtin_amdgcn_s_sleep(8);
        const unsigned off = WS_HP + (unsigned)(((h * 4 + ow) * 8 + r) * 256 + c * 16);
        u32x4 g = EMPTY, g2 = EMPTY;
        for (unsigned spins = 0;; ++spins) {
          asm volatile("" ::: "memory");
          if ((BAD(g) | BAD(g2)) != 0u) { g = GET(off); g2 = GET(off + 1024); }
          if (__all((BAD(g) | BAD(g2)) == 0u) || spins > SPIN_CAP) break;
          __builtin_amdgcn_s_sleep(16);
        }
        float h0 = xrow_sum(__uint_as_float(g[0]) + __uint_as_float(g2[0])), h1 = xrow_sum(__uint_as_float(g[2]) + __uint_as_float(g2[2]));
        h0 = (1.f - g0) * h0 / (1.f + __expf(-h0)); h1 = (1.f - g1) * h1 / (1.f + __expf(-h1));
        if (c >= 14) { h0 = 0.f; h1 = 0.f; }
        lds[L_HC + c] = h0; lds[L_HC + 16 + c] = h1;
        REL_WG();
        lds_st(W + W_HFLAG, 1u);
      }
    } else if (hw == NHW - 2) {
      GAS unsigned long long* ctr = (GAS unsigned long long*)(p.ws + WS_CTR + (h * 2 + b) * 256);
      const unsigned long long t48 = (((unsigned long long)tagB << 32) | ((unsigned long long)(tagA & 0xffffu) << 16));
      while (lds_ld(W + W_QFLAG) == 0u) __builtin_amdgcn_s_sleep(8);
      if (lane == 0) {
        for (unsigned spins = 0; spins < SPIN_CAP; ++spins) {
          unsigned long long v = __hip_atomic_load(ctr, __ATOMIC_RELAXED, __HIP_MEMORY_SCOPE_AGENT);
          if ((v >> 16) == (t48 >> 16)) break;
          if (__hip_atomic_compare_exchange_strong(ctr, &v, t48, __ATOMIC_RELAXED, __ATOMIC_RELAXED, __HIP_MEMORY_SCOPE_AGENT)) break;
        }
      }
#pragma unroll 1
      for (int k = 0; k <= NDYN; ++k) {
        while ((int)lds_ld(W + W_PROG) < STAT + k - AHEAD) __builtin_amdgcn_s_sleep(2);
        unsigned cl = 0u;
        if (lane == 0) cl = (unsigned)__hip_atomic_fetch_add(ctr, 1ull, __ATOMIC_RELAXED, __HIP_MEMORY_SCOPE_AGENT) & 0xffffu;
        cl = __builtin_amdgcn_readfirstlane(cl);
        const bool end = cl >= (unsigned)NDYN || k == NDYN;
        lds_st(DQ + k, end ? DQ_END : (unsigned)(4 * STAT) + cl);
        if (end) break;
      }
      lds_st(W + W_LATE, 1u);
    }
    u32x4 wor[4], w2r[2];
    if (powner) {
      const unsigned* gate_w = W + (hw != 0 ? W_LATE : W_QFLAG);
      while (lds_ld(gate_w) == 0u) __builtin_amdgcn_s_sleep(4);
      asm volatile("" ::: "memory");
      const unsigned short* wop = p.wo + (size_t)(h * DH + 32 * ow + 4 * g8) * D_MODEL + 64 * hw + 8 * c8;
      const unsigned short* w2p = p.w2 + (size_t)(h * FSL + 14 * ow + 2 * (g8 < 7 ? g8 : 6)) * D_MODEL + 64 * hw + 8 * c8;
#pragma unroll
      for (int k = 0; k < 4; ++k) wor[k] = ldnt(wop + k * D_MODEL);
      w2r[0] = ldnt(w2p); w2r[1] = ldnt(w2p + D_MODEL);
      __builtin_amdgcn_sched_barrier(0);
    }
    if (hw == 0) {
      unsigned slot = (unsigned)s;
#pragma unroll 1
      for (int seg = 0; seg < NSEG; ++seg) {
        unsigned nxt;
        while ((nxt = lds_ld(DQ + seg)) == DQ_NONE) __builtin_amdgcn_s_sleep(4);
        nxt = __builtin_amdgcn_readfirstlane(nxt);
        if (nxt == DQ_END) break;
        while (lds_ld(SC + seg) < (unsigned)NSW) __builtin_amdgcn_s_sleep(2);
        ACQ_WG();
        publish_seg(seg, slot, tagA, tagB);
        slot = 4u + nxt - (unsigned)(4 * STAT);
      }
    }
    if (powner) {
      const unsigned pf = WS_FIN + (unsigned)((h * 4 + ow) * 8192 + ((lane >> 3) & 1) * 4096 + (32 * hw + 4 * c8 + 2 * (r & 1) + (r >> 1)) * 16);
      while (lds_ld(W + W_HFLAG) == 0u) __builtin_amdgcn_s_sleep(1);
      ACQ_WG();
      f32x2 y0[4], y1[4], wf[4][4];
      {
        const f32x2 c0 = *(const f32x2*)&lds[L_HC + 2 * g8], c1 = *(const f32x2*)&lds[L_HC + 16 + 2 * g8];
#pragma unroll
        for (int j = 0; j < 4; ++j) {
          const f32x2 wa = {lo16(w2r[0][j]), hi16(w2r[0][j])}, wb = {lo16(w2r[1][j]), hi16(w2r[1][j])};
          y0[j] = fma2(wa, c0[0], wb * c0[1]); y1[j] = fma2(wa, c1[0], wb * c1[1]);
          asm volatile("" : "+v"(y0[j]), "+v"(y1[j]));
#pragma unroll
          for (int k = 0; k < 4; ++k) { wf[k][j] = f32x2{lo16(wor[k][j]), hi16(wor[k][j])}; asm volatile("" : "+v"(wf[k][j])); }
        }
      }
      while (lds_ld(W + W_OFLAG) == 0u) __builtin_amdgcn_s_sleep(1);
      ACQ_WG();
      const f32x4 oa = *(const f32x4*)&lds[L_O + 8 * g8], ob = *(const f32x4*)&lds[L_O + 8 * g8 + 4];
      float yb[8];
#pragma unroll
      for (int j = 0; j < 4; ++j) {
        const f32x2 z0 = fma2(wf[3][j], oa[3], fma2(wf[2][j], oa[2], fma2(wf[1][j], oa[1], fma2(wf[0][j], oa[0], y0[j]))));
        const f32x2 z1 = fma2(wf[3][j], ob[3], fma2(wf[2][j], ob[2], fma2(wf[1][j], ob[1], fma2(wf[0][j], ob[0], y1[j]))));
        const float t0 = z0[0] + dpp<ROR8>(z0[0]), t1 = z1[0] + dpp<ROR8>(z1[0]);
        const float t2 = z0[1] + dpp<ROR8>(z0[1]), t3 = z1[1] + dpp<ROR8>(z1[1]);
        yb[2 * j] = (lane & 8) ? t1 : t0; yb[2 * j + 1] = (lane & 8) ? t3 : t2;
      }
      const f32x2 u = tr8(yb);
      PUT(pf, u[0], u[1]);
    }
  }
}

extern "C" void kernel_launch(void* const* d_in, const int* in_sizes, int n_in, void* d_out, int out_size, void* d_ws, size_t ws_size,
                              hipStream_t stream) {
  Params p;
  p.x = (const unsigned short*)d_in[0];
  p.K = (const unsigned short*)d_in[1];
  p.V = (const unsigned short*)d_in[2];
  p.wq = (const unsigned short*)d_in[3];
  p.wo = (const unsigned short*)d_in[4];
  p.w1 = (const unsigned short*)d_in[6];
  p.w2 = (const unsigned short*)d_in[7];
  p.gw = (const unsigned short*)d_in[8];
  p.out = (unsigned short*)d_out;
  p.ws = (unsigned char*)d_ws;
  fused_decode_kernel<<<256, NTHR, 0, stream>>>(p);
}
```
